```python
import torch

def _softmax(x, axis=-1):
    return torch.softmax(x, dim=axis)
def _gelu(x):
    return torch.nn.functional.gelu(x, approximate="tanh")
def _silu(x):
    return torch.nn.functional.silu(x)
def _sigmoid(x):
    return torch.sigmoid(x)
def _layernorm(x, axis=-1, eps=1e-5):
    m = x.mean(dim=axis, keepdim=True)
    v = x.var(dim=axis, keepdim=True, unbiased=False)
    return (x - m) / torch.sqrt(v + eps)
def _scatter_add(tgt, idx, src, axis=0):
    return tgt.clone().index_add_(axis, idx, src)
def _segment_sum(src, seg, n):
    out = torch.zeros((n,) + src.shape[1:], dtype=src.dtype)
    return out.index_add_(0, seg, src)
def _pad(x, pw):
    flat = tuple(v for pair in reversed(pw) for v in pair)
    return torch.nn.functional.pad(x, flat)

kernel_name = 'standalone__kv_cache_quant_int4_int2_kv_with_per_channel_or_per_token_scales__sampling'

def setup_inputs():

    _w = (lambda a: (lambda t: t.to(torch.bfloat16) if t.is_floating_point() else t)(torch.as_tensor(a)))
    B = 32
    Sq = 4
    Skv = 2048
    Hq = 32
    Hkv = 16
    Dh = 128
    bits = 8
    G = 64
    if G <= 0 or G >= Skv:
        G = max(1, Skv // 2)
    Skv = (Skv // G) * G
    ngk = Skv // G
    qmax = (1 << bits) - 1
    return {
        "q": _w((lambda *s: torch.randn(*s, dtype=torch.bfloat16) * 0.2)(B, Hq, Sq, Dh)),
        "qk": _w(torch.randint(0, qmax + 1, (B, Hkv, Skv, Dh)).to(torch.int32)),
        "qv": _w(torch.randint(0, qmax + 1, (B, Hkv, Skv, Dh)).to(torch.int32)),
        "k_scales": _w(torch.abs((lambda *s: torch.randn(*s, dtype=torch.bfloat16) * 0.2)(B, Hkv, ngk, Dh)) * 0.05 + 0.02),
        "k_zeros": _w((lambda *s: torch.randn(*s, dtype=torch.bfloat16) * 0.2)(B, Hkv, ngk, Dh) + float(qmax) / 2.0),
        "v_scales": _w(torch.abs((lambda *s: torch.randn(*s, dtype=torch.bfloat16) * 0.05)(B, Hkv, Skv, 1)) * 0.05 + 0.02),
        "v_zeros": _w((lambda *s: torch.randn(*s, dtype=torch.bfloat16) * 0.05)(B, Hkv, Skv, 1) + float(qmax) / 2.0),
    }

def reference(q, qk, qv, k_scales, k_zeros, v_scales, v_zeros):
    B, Hq, Sq, Dh = q.shape
    Hkv, Skv = qk.shape[1], qk.shape[2]
    ngk = k_scales.shape[2]
    G = Skv // ngk
    ks = (lambda x, n, axis: torch.repeat_interleave(x, n, dim=axis))(k_scales, G, axis=2)
    kz = (lambda x, n, axis: torch.repeat_interleave(x, n, dim=axis))(k_zeros, G, axis=2)
    k = ((lambda x, dt: x.to(dt))(qk, q.dtype) - kz) * ks
    v = ((lambda x, dt: x.to(dt))(qv, q.dtype) - v_zeros) * v_scales
    rep = Hq // Hkv
    if rep > 1:
        k = (lambda x, n, axis: torch.repeat_interleave(x, n, dim=axis))(k, rep, axis=1)
        v = (lambda x, n, axis: torch.repeat_interleave(x, n, dim=axis))(v, rep, axis=1)
    scale = float(Dh) ** -0.5
    logits = torch.einsum("bhqd,bhkd->bhqk", q, k) * scale
    p = _softmax(logits, axis=-1)
    o = torch.einsum("bhqk,bhkd->bhqd", p, v)
    return o
```

```cpp
#include <hip/hip_runtime.h>
#include <hip/hip_bf16.h>
#include <cstdio>

#define LAS __attribute__((address_space(3)))
#define GAS __attribute__((address_space(1)))
typedef int      i32x4  __attribute__((ext_vector_type(4)));
typedef unsigned u32x4  __attribute__((ext_vector_type(4)));
typedef unsigned u32x2  __attribute__((ext_vector_type(2)));
typedef float    f32x4  __attribute__((ext_vector_type(4)));
typedef float    f32x2  __attribute__((ext_vector_type(2)));
typedef __bf16   bf16x8 __attribute__((ext_vector_type(8)));
typedef __bf16   bf16x2 __attribute__((ext_vector_type(2)));

#define KV_WIN 20

constexpr int NB = 32, HQ = 32, SQ = 4, SKV = 2048, HKV = 16, DH = 128, GSZ = 64, NGK = SKV / GSZ;
constexpr int NPAIR = NB * HKV;
constexpr int MROWS = (HQ / HKV) * SQ;
constexpr int NWG = 256;
constexpr int PPW = NPAIR / NWG;
constexpr int NGRP = PPW * NGK;
constexpr int NCONS = 3;
constexpr int NTHR = (1 + NCONS) * 64;
constexpr int WIN = KV_WIN;
constexpr int NSLOT = 8, SLOT_BYTES = 16384;
constexpr int GLOADS = 65;
constexpr int LDS_RING = 0;
constexpr int LDS_META = NSLOT * SLOT_BYTES;
constexpr int MRG_REC = MROWS * DH * 4 + 64;
constexpr int LDS_MRG = LDS_META + 4 * 1024;
constexpr int LDS_FLAG = LDS_MRG + PPW * NCONS * MRG_REC;
constexpr int LDS_BYTES = LDS_FLAG + 128;
static_assert(LDS_BYTES <= 160 * 1024, "LDS");
static_assert(WIN >= 2 && WIN <= 48, "window");
constexpr int SPIN_CAP = 1 << 22;

template <int N> __device__ __forceinline__ void wait_vm() { asm volatile("s_waitcnt vmcnt(%0)" ::"i"(N) : "memory"); }
__device__ __forceinline__ void wait_lgkm0() { asm volatile("s_waitcnt lgkmcnt(0)" ::: "memory"); }
__device__ __forceinline__ void set_m0(unsigned v) { asm volatile("s_mov_b32 m0, %0\n\ts_nop 0" ::"s"(v) : "memory"); }
template <int IMM> __device__ __forceinline__ void dma_buf(i32x4 rsrc, unsigned voff, unsigned soff) {
  asm volatile("buffer_load_dwordx4 %0, %1, %2 offen offset:%3 nt lds" ::"v"(voff), "s"(rsrc), "s"(soff), "i"(IMM) : "memory");
}
__device__ __forceinline__ void dma_glb(const void* p) {
  asm volatile("global_load_lds_dwordx4 %0, off nt" ::"v"(p) : "memory");
}
__device__ __forceinline__ unsigned rfl(unsigned v) { return __builtin_amdgcn_readfirstlane(v); }
__device__ __forceinline__ unsigned pk_bf16(float lo, float hi) {
  bf16x2 r = {(__bf16)lo, (__bf16)hi};
  return __builtin_bit_cast(unsigned, r);
}
__device__ __forceinline__ float bf_lo(unsigned w) { return __builtin_bit_cast(float, w << 16); }
__device__ __forceinline__ float bf_hi(unsigned w) { return __builtin_bit_cast(float, w & 0xffff0000u); }
__device__ __forceinline__ f32x4 mfma16(u32x4 a, u32x4 b, f32x4 c) {
  return __builtin_amdgcn_mfma_f32_16x16x32_bf16(__builtin_bit_cast(bf16x8, a), __builtin_bit_cast(bf16x8, b), c, 0, 0, 0);
}
__device__ __forceinline__ float xg_max(float x) {
  auto s = __builtin_amdgcn_permlane16_swap(__float_as_uint(x), __float_as_uint(x), false, false);
  x = fmaxf(__uint_as_float(s[0]), __uint_as_float(s[1]));
  auto t = __builtin_amdgcn_permlane32_swap(__float_as_uint(x), __float_as_uint(x), false, false);
  return fmaxf(__uint_as_float(t[0]), __uint_as_float(t[1]));
}
__device__ __forceinline__ float xg_sum(float x) {
  auto s = __builtin_amdgcn_permlane16_swap(__float_as_uint(x), __float_as_uint(x), false, false);
  x = __uint_as_float(s[0]) + __uint_as_float(s[1]);
  auto t = __builtin_amdgcn_permlane32_swap(__float_as_uint(x), __float_as_uint(x), false, false);
  return __uint_as_float(t[0]) + __uint_as_float(t[1]);
}

struct Args {
  const __hip_bfloat16* q; const int* qk; const int* qv;
  const __hip_bfloat16* ks; const __hip_bfloat16* kz; const __hip_bfloat16* vs; const __hip_bfloat16* vz;
  __hip_bfloat16* out;
};

template <int Q> struct LoaderStep {
  static __device__ __forceinline__ void run(int G, i32x4 rk, i32x4 rv, const unsigned (&voffk)[8], unsigned voffv, unsigned gbase,
                                             const void* metap, volatile LAS unsigned* FULL, volatile LAS unsigned* FREE, unsigned ldsb, bool& dead) {
    if constexpr (Q >= 1 && ((Q - 1) & 15) == 0) {
      constexpr int s = (Q - 1) >> 4;
      if (G >= 2) {
        const unsigned need = (unsigned)(4 * G + s - NSLOT + 1);
        const int ps = (G & 1) * 4 + s;
        if (rfl(FREE[ps]) != need) {
          int sp = 0;
          while (rfl(FREE[ps]) != need) { __builtin_amdgcn_s_sleep(1); if (++sp > SPIN_CAP) { dead = true; break; } }
        }
      }
    }
    wait_vm<WIN - 1>();
#pragma unroll
    for (int s = 0; s < 4; ++s) {
      if (16 * (s + 1) + WIN == Q) FULL[(G & 1) * 4 + s] = (unsigned)(4 * G + s + 1);
      if (16 * (s + 1) + WIN - GLOADS == Q) { if (G >= 1) FULL[((G - 1) & 1) * 4 + s] = (unsigned)(4 * (G - 1) + s + 1); }
    }
    if constexpr (Q == 0) {
      set_m0(ldsb + LDS_META + (G & 3) * 1024);
      dma_glb(metap);
    } else {
      constexpr int s = (Q - 1) >> 4, i = (Q - 1) & 15;
      constexpr bool isV = s >= 2;
      const unsigned soff = gbase + (unsigned)((s & 1) * SLOT_BYTES + (i >> 2) * 4096);
      if constexpr ((i & 3) == 0) set_m0(ldsb + LDS_RING + ((G & 1) * 4 + s) * SLOT_BYTES + (i >> 2) * 4096);
      if constexpr (isV) dma_buf<(i & 3) * 1024>(rv, voffv, soff);
      else dma_buf<(i & 3) * 1024>(rk, voffk[i & 7], soff);
    }
    if constexpr (Q == WIN - 1) {
      if (G == 0) __builtin_amdgcn_s_barrier();
    }
    if constexpr (Q + 1 < GLOADS) LoaderStep<Q + 1>::run(G, rk, rv, voffk, voffv, gbase, metap, FULL, FREE, ldsb, dead);
  }
};

__device__ __forceinline__ bool wait_full(volatile LAS unsigned* FULL, int ps, unsigned need) {
  int sp = 0;
  while (rfl(FULL[ps]) != need) { __builtin_amdgcn_s_sleep(1); if (++sp > SPIN_CAP) return false; }
  asm volatile("" ::: "memory");
  return true;
}

__global__ void __launch_bounds__(NTHR, 1) kv_attn_kernel(Args a) {
  __shared__ __attribute__((aligned(16))) unsigned char lds[LDS_BYTES];
  LAS unsigned char* L = (LAS unsigned char*)lds;
  volatile LAS unsigned* FULL = (volatile LAS unsigned*)(L + LDS_FLAG);
  volatile LAS unsigned* FREE = FULL + 8;
  LAS unsigned* CNT = (LAS unsigned*)(L + LDS_FLAG) + 16;
  const int tid = threadIdx.x, lane = tid & 63, wave = __builtin_amdgcn_readfirstlane(tid >> 6);
  const int wg = blockIdx.x;
  const unsigned ldsb = (unsigned)(size_t)L;

  if (wave == 0) {
    i32x4 rk, rv;
    rk.x = (int)(unsigned)(size_t)a.qk; rk.y = (int)(unsigned)((size_t)a.qk >> 32); rk.z = NPAIR * SKV * DH * 4; rk.w = 0x00020000;
    rv.x = (int)(unsigned)(size_t)a.qv; rv.y = (int)(unsigned)((size_t)a.qv >> 32); rv.z = NPAIR * SKV * DH * 4; rv.w = 0x00020000;
    unsigned voffk[8];
#pragma unroll
    for (int i = 0; i < 8; ++i) voffk[i] = (unsigned)((lane >> 5) * 512 + (((lane & 31) ^ ((2 * i + (lane >> 5)) & 15)) << 4));
    const unsigned voffv = (unsigned)lane * 16;
    const int g0 = wg * NGRP;
    const char* metap; unsigned mstride;
    if (lane < 16)      { metap = (const char*)a.ks + (size_t)g0 * 256 + lane * 16;        mstride = 256; }
    else if (lane < 32) { metap = (const char*)a.kz + (size_t)g0 * 256 + (lane - 16) * 16; mstride = 256; }
    else if (lane >= 40 && lane < 48) { metap = (const char*)a.vz + (size_t)g0 * 128 + (lane - 40) * 16; mstride = 128; }
    else                { metap = (const char*)a.vs + (size_t)g0 * 128 + (lane & 7) * 16;  mstride = 128; }
    unsigned gbase = (unsigned)g0 * (GSZ * DH * 4);
    bool dead = false;
    for (int G = 0; G < NGRP; ++G) {
      LoaderStep<0>::run(G, rk, rv, voffk, voffv, gbase, metap, FULL, FREE, ldsb, dead);
      gbase += GSZ * DH * 4;
      metap += mstride;
      if (dead) break;
    }
    constexpr int GL = NGRP - 1;
    if (!dead) {
#pragma unroll
      for (int s = 0; s < 4; ++s) {
        if (16 * (s + 1) + WIN > GLOADS - 1) {
          if (s == 0) wait_vm<48>(); else if (s == 1) wait_vm<32>(); else if (s == 2) wait_vm<16>(); else wait_vm<0>();
          FULL[(GL & 1) * 4 + s] = (unsigned)(4 * GL + s + 1);
        }
      }
    }
    wait_vm<0>();
    return;
  }

  const int ci = wave - 1;
  const int r = lane & 15, g = lane >> 4;
  if (wave == 1 && lane < 32) ((LAS unsigned*)(L + LDS_FLAG))[lane] = 0u;
  wait_lgkm0();
  __builtin_amdgcn_s_barrier();
  asm volatile("" ::: "memory");

  constexpr float C2 = 0.08838834764831845f * 1.4426950408889634f;
  unsigned koff[8];
#pragma unroll
  for (int i = 0; i < 8; ++i) koff[i] = (unsigned)(r * 512 + (((8 * (i >> 1) + 2 * g + (i & 1)) ^ r) << 4));
  const unsigned voff = (unsigned)(4 * g * 512 + r * 16);

  for (int pr = 0; pr < PPW; ++pr) {
    const int pair = wg * PPW + pr;
    float qf[32];
    {
      const GAS u32x4* qp = (const GAS u32x4*)((const char*)a.q + ((size_t)(pair * MROWS + (r & 7)) * DH + 8 * g) * 2);
#pragma unroll
      for (int s = 0; s < 4; ++s) {
        const u32x4 w = qp[4 * s];
#pragma unroll
        for (int k = 0; k < 4; ++k) { qf[8 * s + 2 * k] = bf_lo(w[k]); qf[8 * s + 2 * k + 1] = bf_hi(w[k]); }
      }
    }
    float M = -INFINITY, lsum = 0.f;
    f32x4 O[8], Dc = {0.f, 0.f, 0.f, 0.f};
#pragma unroll
    for (int i = 0; i < 8; ++i) O[i] = f32x4{0.f, 0.f, 0.f, 0.f};

    const int Gbeg = NGK * pr + ((ci + NCONS - (NGK * pr) % NCONS) % NCONS);
    bool ok = true;
    for (int G = Gbeg; G < NGK * (pr + 1); G += NCONS) {
      const int ps = (G & 1) * 4;
      const unsigned n0 = (unsigned)(4 * G);
      LAS unsigned char* meta = L + LDS_META + (G & 3) * 1024;
      if (!wait_full(FULL, ps, n0 + 1)) { ok = false; break; }
      u32x4 bq[4];
      f32x4 Dz = {0.f, 0.f, 0.f, 0.f};
#pragma unroll
      for (int s = 0; s < 4; ++s) {
        const u32x4 w = *(const LAS u32x4*)(meta + (32 * s + 8 * g) * 2);
        const u32x4 z = *(const LAS u32x4*)(meta + 256 + (32 * s + 8 * g) * 2);
#pragma unroll
        for (int k = 0; k < 4; ++k) bq[s][k] = pk_bf16(qf[8 * s + 2 * k] * bf_lo(w[k]), qf[8 * s + 2 * k + 1] * bf_hi(w[k]));
        Dz = mfma16(z, bq[s], Dz);
      }
      u32x2 vsw[4]; u32x4 vzf[2];
#pragma unroll
      for (int t = 0; t < 4; ++t) vsw[t] = *(const LAS u32x2*)(meta + 512 + (16 * t + 4 * g) * 2);
#pragma unroll
      for (int u = 0; u < 2; ++u) {
        const u32x2 lo = *(const LAS u32x2*)(meta + 640 + (32 * u + 4 * g) * 2);
        const u32x2 hi = *(const LAS u32x2*)(meta + 640 + (32 * u + 16 + 4 * g) * 2);
        vzf[u] = u32x4{lo[0], lo[1], hi[0], hi[1]};
      }
      f32x4 S[4];
#pragma unroll
      for (int u = 0; u < 2; ++u) {
        if (u == 1) { if (!wait_full(FULL, ps + 1, n0 + 2)) { ok = false; break; } }
        LAS unsigned char* slot = L + LDS_RING + (ps + u) * SLOT_BYTES;
#pragma unroll
        for (int tt = 0; tt < 2; ++tt) {
          i32x4 x[8];
#pragma unroll
          for (int i = 0; i < 8; ++i) x[i] = *(const LAS i32x4*)(slot + tt * 8192 + koff[i]);
          f32x4 acc = {0.f, 0.f, 0.f, 0.f};
#pragma unroll
          for (int s = 0; s < 4; ++s) {
            const i32x4 x0 = x[2 * s], x1 = x[2 * s + 1];
            const u32x4 af = {pk_bf16((float)x0[0], (float)x0[1]), pk_bf16((float)x0[2], (float)x0[3]),
                              pk_bf16((float)x1[0], (float)x1[1]), pk_bf16((float)x1[2], (float)x1[3])};
            acc = mfma16(af, bq[s], acc);
          }
          S[2 * u + tt] = acc;
        }
        wait_lgkm0();
        FREE[ps + u] = n0 + u + 1;
      }
      if (!ok) break;
      float gm = fmaxf(fmaxf(S[0][0], S[0][1]), fmaxf(S[0][2], S[0][3]));
#pragma unroll
      for (int t = 1; t < 4; ++t) gm = fmaxf(gm, fmaxf(fmaxf(S[t][0], S[t][1]), fmaxf(S[t][2], S[t][3])));
      const float dz = Dz[0];
      gm = xg_max(gm) - dz;
      const float Mn = fmaxf(M, gm);
      if (__any(Mn > M)) {
        const float alpha = __builtin_amdgcn_exp2f((M - Mn) * C2);
        lsum *= alpha;
#pragma unroll
        for (int i = 0; i < 8; ++i) O[i] *= alpha;
        Dc *= alpha;
        M = Mn;
      }
      const float bias = (M + dz) * C2;
      u32x4 pb[2];
#pragma unroll
      for (int t = 0; t < 4; ++t) {
        float p[4];
#pragma unroll
        for (int i = 0; i < 4; ++i) { p[i] = __builtin_amdgcn_exp2f(fmaf(S[t][i], C2, -bias)); lsum += p[i]; }
        pb[t >> 1][2 * (t & 1)]     = pk_bf16(p[0] * bf_lo(vsw[t][0]), p[1] * bf_hi(vsw[t][0]));
        pb[t >> 1][2 * (t & 1) + 1] = pk_bf16(p[2] * bf_lo(vsw[t][1]), p[3] * bf_hi(vsw[t][1]));
      }
#pragma unroll
      for (int u = 0; u < 2; ++u) {
        if (!wait_full(FULL, ps + 2 + u, n0 + 3 + u)) { ok = false; break; }
        LAS unsigned char* slot = L + LDS_RING + (ps + 2 + u) * SLOT_BYTES;
#pragma unroll
        for (int h = 0; h < 2; ++h) {
          i32x4 x[8];
#pragma unroll
          for (int j = 0; j < 8; ++j) x[j] = *(const LAS i32x4*)(slot + voff + ((j & 3) + 16 * (j >> 2)) * 512 + h * 256);
#pragma unroll
          for (int e = 0; e < 4; ++e) {
            const u32x4 af = {pk_bf16((float)x[0][e], (float)x[1][e]), pk_bf16((float)x[2][e], (float)x[3][e]),
                              pk_bf16((float)x[4][e], (float)x[5][e]), pk_bf16((float)x[6][e], (float)x[7][e])};
            O[4 * h + e] = mfma16(af, pb[u], O[4 * h + e]);
          }
        }
        Dc = mfma16(vzf[u], pb[u], Dc);
        wait_lgkm0();
        FREE[ps + 2 + u] = n0 + 3 + u;
      }
      if (!ok) break;
    }
    const float ltot = xg_sum(lsum);
    LAS unsigned char* rec = L + LDS_MRG + (pr * NCONS + ci) * MRG_REC;
    if (r < 8) {
      const float dc = Dc[0];
#pragma unroll
      for (int h = 0; h < 2; ++h)
#pragma unroll
        for (int i = 0; i < 4; ++i)
          *(LAS f32x4*)(rec + (r * DH + 64 * h + 16 * g + 4 * i) * 4) =
              f32x4{O[4 * h + 0][i] - dc, O[4 * h + 1][i] - dc, O[4 * h + 2][i] - dc, O[4 * h + 3][i] - dc};
      if (g == 0) { *(LAS float*)(rec + MROWS * DH * 4 + r * 4) = M; *(LAS float*)(rec + MROWS * DH * 4 + 32 + r * 4) = ltot; }
    }
    wait_lgkm0();
    unsigned old = 0;
    if (lane == 0) old = __hip_atomic_fetch_add(CNT + pr, 1u, __ATOMIC_RELAXED, __HIP_MEMORY_SCOPE_WORKGROUP);
    old = rfl(old);
    asm volatile("" ::: "memory");
    if (old == NCONS - 1) {
      const int m = lane >> 3, d0 = (lane & 7) * 16;
      LAS unsigned char* rb = L + LDS_MRG + pr * NCONS * MRG_REC;
      float Mc[NCONS], Lc[NCONS], Mx = -INFINITY;
#pragma unroll
      for (int c = 0; c < NCONS; ++c) {
        Mc[c] = *(const LAS float*)(rb + c * MRG_REC + MROWS * DH * 4 + m * 4);
        Lc[c] = *(const LAS float*)(rb + c * MRG_REC + MROWS * DH * 4 + 32 + m * 4);
        Mx = fmaxf(Mx, Mc[c]);
      }
      float w[NCONS], Lt = 0.f;
#pragma unroll
      for (int c = 0; c < NCONS; ++c) { w[c] = __builtin_amdgcn_exp2f((Mc[c] - Mx) * C2); Lt = fmaf(w[c], Lc[c], Lt); }
      const float inv = 1.f / Lt;
      u32x4 ov[2];
#pragma unroll
      for (int k = 0; k < 4; ++k) {
        f32x4 acc = {0.f, 0.f, 0.f, 0.f};
#pragma unroll
        for (int c = 0; c < NCONS; ++c) acc += *(const LAS f32x4*)(rb + c * MRG_REC + (m * DH + d0 + 4 * k) * 4) * w[c];
        acc *= inv;
        ov[k >> 1][2 * (k & 1)] = pk_bf16(acc[0], acc[1]);
        ov[k >> 1][2 * (k & 1) + 1] = pk_bf16(acc[2], acc[3]);
      }
      GAS u32x4* op = (GAS u32x4*)((char*)a.out + ((size_t)(pair * MROWS + m) * DH + d0) * 2);
      op[0] = ov[0]; op[1] = ov[1];
    }
    if (!ok) break;
  }
}

extern "C" void kernel_launch(void* const* d_in, const int* in_sizes, int n_in, void* d_out, int out_size, void* d_ws, size_t ws_size,
                              hipStream_t stream) {
  (void)d_ws; (void)ws_size;
  if (n_in != 7 || in_sizes[0] != NB * HQ * SQ * DH || in_sizes[1] != NPAIR * SKV * DH || in_sizes[2] != NPAIR * SKV * DH ||
      in_sizes[3] != NPAIR * NGK * DH || in_sizes[4] != NPAIR * NGK * DH || in_sizes[5] != NPAIR * SKV || in_sizes[6] != NPAIR * SKV ||
      out_size != NB * HQ * SQ * DH) {
    fprintf(stderr, "kernel_launch: unexpected shapes\n");
    return;
  }
  Args a;
  a.q = (const __hip_bfloat16*)d_in[0]; a.qk = (const int*)d_in[1]; a.qv = (const int*)d_in[2];
  a.ks = (const __hip_bfloat16*)d_in[3]; a.kz = (const __hip_bfloat16*)d_in[4];
  a.vs = (const __hip_bfloat16*)d_in[5]; a.vz = (const __hip_bfloat16*)d_in[6];
  a.out = (__hip_bfloat16*)d_out;
  kv_attn_kernel<<<NWG, NTHR, 0, stream>>>(a);
}
```

```cpp
#include <hip/hip_runtime.h>
#include <hip/hip_bf16.h>
#include <cstdio>

#define LAS __attribute__((address_space(3)))
#define GAS __attribute__((address_space(1)))
typedef int      i32x4  __attribute__((ext_vector_type(4)));
typedef unsigned u32x4  __attribute__((ext_vector_type(4)));
typedef unsigned u32x2  __attribute__((ext_vector_type(2)));
typedef float    f32x4  __attribute__((ext_vector_type(4)));
typedef float    f32x2  __attribute__((ext_vector_type(2)));
typedef __bf16   bf16x8 __attribute__((ext_vector_type(8)));
typedef __bf16   bf16x2 __attribute__((ext_vector_type(2)));

#define KV_WIN 20

constexpr int NB = 32, HQ = 32, SQ = 4, SKV = 2048, HKV = 16, DH = 128, GSZ = 64, NGK = SKV / GSZ;
constexpr int NPAIR = NB * HKV;
constexpr int MROWS = (HQ / HKV) * SQ;
constexpr int NWG = 256;
constexpr int PPW = NPAIR / NWG;
constexpr int NGRP = PPW * NGK;
constexpr int NCONS = 3;
constexpr int NTHR = (1 + NCONS) * 64;
constexpr int WIN = KV_WIN;
constexpr int NSLOT = 8, SLOT_BYTES = 16384;
constexpr int GLOADS = 65;
constexpr int LDS_RING = 0;
constexpr int LDS_META = NSLOT * SLOT_BYTES;
constexpr int MRG_REC = MROWS * DH * 4 + 64;
constexpr int LDS_MRG = LDS_META + 4 * 1024;
constexpr int LDS_FLAG = LDS_MRG + PPW * NCONS * MRG_REC;
constexpr int LDS_BYTES = LDS_FLAG + 128;
static_assert(LDS_BYTES <= 160 * 1024, "LDS");
static_assert(WIN >= 2 && WIN <= 48, "window");
constexpr int SPIN_CAP = 1 << 22;

template <int N> __device__ __forceinline__ void wait_vm() { asm volatile("s_waitcnt vmcnt(%0)" ::"i"(N) : "memory"); }
__device__ __forceinline__ void wait_lgkm0() { asm volatile("s_waitcnt lgkmcnt(0)" ::: "memory"); }
__device__ __forceinline__ void set_m0(unsigned v) { asm volatile("s_mov_b32 m0, %0\n\ts_nop 0" ::"s"(v) : "memory"); }
template <int IMM> __device__ __forceinline__ void dma_buf(i32x4 rsrc, unsigned voff, unsigned soff) {
  asm volatile("buffer_load_dwordx4 %0, %1, %2 offen offset:%3 nt lds" ::"v"(voff), "s"(rsrc), "s"(soff), "i"(IMM) : "memory");
}
__device__ __forceinline__ void dma_glb(const void* p) {
  asm volatile("global_load_lds_dwordx4 %0, off nt" ::"v"(p) : "memory");
}
__device__ __forceinline__ unsigned rfl(unsigned v) { return __builtin_amdgcn_readfirstlane(v); }
__device__ __forceinline__ unsigned pk_bf16(float lo, float hi) {
  bf16x2 r = {(__bf16)lo, (__bf16)hi};
  return __builtin_bit_cast(unsigned, r);
}
__device__ __forceinline__ float bf_lo(unsigned w) { return __builtin_bit_cast(float, w << 16); }
__device__ __forceinline__ float bf_hi(unsigned w) { return __builtin_bit_cast(float, w & 0xffff0000u); }
__device__ __forceinline__ f32x4 mfma16(u32x4 a, u32x4 b, f32x4 c) {
  return __builtin_amdgcn_mfma_f32_16x16x32_bf16(__builtin_bit_cast(bf16x8, a), __builtin_bit_cast(bf16x8, b), c, 0, 0, 0);
}
__device__ __forceinline__ float xg_max(float x) {
  auto s = __builtin_amdgcn_permlane16_swap(__float_as_uint(x), __float_as_uint(x), false, false);
  x = fmaxf(__uint_as_float(s[0]), __uint_as_float(s[1]));
  auto t = __builtin_amdgcn_permlane32_swap(__float_as_uint(x), __float_as_uint(x), false, false);
  return fmaxf(__uint_as_float(t[0]), __uint_as_float(t[1]));
}
__device__ __forceinline__ float xg_sum(float x) {
  auto s = __builtin_amdgcn_permlane16_swap(__float_as_uint(x), __float_as_uint(x), false, false);
  x = __uint_as_float(s[0]) + __uint_as_float(s[1]);
  auto t = __builtin_amdgcn_permlane32_swap(__float_as_uint(x), __float_as_uint(x), false, false);
  return __uint_as_float(t[0]) + __uint_as_float(t[1]);
}

struct Args {
  const __hip_bfloat16* q; const int* qk; const int* qv;
  const __hip_bfloat16* ks; const __hip_bfloat16* kz; const __hip_bfloat16* vs; const __hip_bfloat16* vz;
  __hip_bfloat16* out;
};

template <int Q> struct LoaderStep {
  static __device__ __forceinline__ void run(int G, i32x4 rk, i32x4 rv, const unsigned (&voffk)[8], unsigned voffv, unsigned gbase,
                                             const void* metap, volatile LAS unsigned* FULL, volatile LAS unsigned* FREE, unsigned ldsb, int& budget) {
    if constexpr (Q >= 1 && ((Q - 1) & 15) == 0) {
      constexpr int s = (Q - 1) >> 4;
      if (G >= 2) {
        const unsigned need = (unsigned)(4 * G + s - NSLOT + 1);
        const int ps = (G & 1) * 4 + s;
        if (rfl(FREE[ps]) != need) {
          while (rfl(FREE[ps]) != need && budget > 0) { __builtin_amdgcn_s_sleep(1); --budget; }
        }
      }
    }
    wait_vm<WIN - 1>();
#pragma unroll
    for (int s = 0; s < 4; ++s) {
      if (16 * (s + 1) + WIN == Q) FULL[(G & 1) * 4 + s] = (unsigned)(4 * G + s + 1);
      if (16 * (s + 1) + WIN - GLOADS == Q) { if (G >= 1) FULL[((G - 1) & 1) * 4 + s] = (unsigned)(4 * (G - 1) + s + 1); }
    }
    if constexpr (Q == 0) {
      set_m0(ldsb + LDS_META + (G & 3) * 1024);
      dma_glb(metap);
    } else {
      constexpr int s = (Q - 1) >> 4, i = (Q - 1) & 15;
      constexpr bool isV = s >= 2;
      const unsigned soff = gbase + (unsigned)((s & 1) * SLOT_BYTES + (i >> 2) * 4096);
      if constexpr ((i & 3) == 0) set_m0(ldsb + LDS_RING + ((G & 1) * 4 + s) * SLOT_BYTES + (i >> 2) * 4096);
      if constexpr (isV) dma_buf<(i & 3) * 1024>(rv, voffv, soff);
      else dma_buf<(i & 3) * 1024>(rk, voffk[i & 7], soff);
    }
    if constexpr (Q == WIN - 1) {
      if (G == 0) __builtin_amdgcn_s_barrier();
    }
    if constexpr (Q + 1 < GLOADS) LoaderStep<Q + 1>::run(G, rk, rv, voffk, voffv, gbase, metap, FULL, FREE, ldsb, budget);
  }
};

__device__ __forceinline__ void wait_full(volatile LAS unsigned* FULL, int ps, unsigned need, int& budget) {
  while (rfl(FULL[ps]) != need && budget > 0) { __builtin_amdgcn_s_sleep(1); --budget; }
  asm volatile("" ::: "memory");
}

__global__ void __launch_bounds__(NTHR, 1) kv_attn_kernel(Args a) {
  __shared__ __attribute__((aligned(16))) unsigned char lds[LDS_BYTES];
  LAS unsigned char* L = (LAS unsigned char*)lds;
  volatile LAS unsigned* FULL = (volatile LAS unsigned*)(L + LDS_FLAG);
  volatile LAS unsigned* FREE = FULL + 8;
  LAS unsigned* CNT = (LAS unsigned*)(L + LDS_FLAG) + 16;
  const int tid = threadIdx.x, lane = tid & 63, wave = __builtin_amdgcn_readfirstlane(tid >> 6);
  const int wg = blockIdx.x;
  const unsigned ldsb = (unsigned)(size_t)L;

  if (wave == 0) {
    i32x4 rk, rv;
    rk.x = (int)(unsigned)(size_t)a.qk; rk.y = (int)(unsigned)((size_t)a.qk >> 32); rk.z = NPAIR * SKV * DH * 4; rk.w = 0x00020000;
    rv.x = (int)(unsigned)(size_t)a.qv; rv.y = (int)(unsigned)((size_t)a.qv >> 32); rv.z = NPAIR * SKV * DH * 4; rv.w = 0x00020000;
    unsigned voffk[8];
#pragma unroll
    for (int i = 0; i < 8; ++i) voffk[i] = (unsigned)((lane >> 5) * 512 + (((lane & 31) ^ ((2 * i + (lane >> 5)) & 15)) << 4));
    const unsigned voffv = (unsigned)lane * 16;
    const int g0 = wg * NGRP;
    const char* metap; unsigned mstride;
    if (lane < 16)      { metap = (const char*)a.ks + (size_t)g0 * 256 + lane * 16;        mstride = 256; }
    else if (lane < 32) { metap = (const char*)a.kz + (size_t)g0 * 256 + (lane - 16) * 16; mstride = 256; }
    else if (lane >= 40 && lane < 48) { metap = (const char*)a.vz + (size_t)g0 * 128 + (lane - 40) * 16; mstride = 128; }
    else                { metap = (const char*)a.vs + (size_t)g0 * 128 + (lane & 7) * 16;  mstride = 128; }
    unsigned gbase = (unsigned)g0 * (GSZ * DH * 4);
    int budget = SPIN_CAP;
    for (int G = 0; G < NGRP; ++G) {
      LoaderStep<0>::run(G, rk, rv, voffk, voffv, gbase, metap, FULL, FREE, ldsb, budget);
      gbase += GSZ * DH * 4;
      metap += mstride;
    }
    constexpr int GL = NGRP - 1;
#pragma unroll
    for (int s = 0; s < 4; ++s) {
      if (16 * (s + 1) + WIN > GLOADS - 1) {
        if (s == 0) wait_vm<48>(); else if (s == 1) wait_vm<32>(); else if (s == 2) wait_vm<16>(); else wait_vm<0>();
        FULL[(GL & 1) * 4 + s] = (unsigned)(4 * GL + s + 1);
      }
    }
    return;
  }

  const int ci = wave - 1;
  const int r = lane & 15, g = lane >> 4;
  if (wave == 1 && lane < 32) ((LAS unsigned*)(L + LDS_FLAG))[lane] = 0u;
  wait_lgkm0();
  __builtin_amdgcn_s_barrier();
  asm volatile("" ::: "memory");

  constexpr float C2 = 0.08838834764831845f * 1.4426950408889634f;
  unsigned koff[8];
#pragma unroll
  for (int i = 0; i < 8; ++i) koff[i] = (unsigned)(r * 512 + (((8 * (i >> 1) + 2 * g + (i & 1)) ^ r) << 4));
  const unsigned voff = (unsigned)(4 * g * 512 + r * 16);

  int budget = SPIN_CAP;
  for (int pr = 0; pr < PPW; ++pr) {
    const int pair = wg * PPW + pr;
    float qf[32];
    {
      const GAS u32x4* qp = (const GAS u32x4*)((const char*)a.q + ((size_t)(pair * MROWS + (r & 7)) * DH + 8 * g) * 2);
#pragma unroll
      for (int s = 0; s < 4; ++s) {
        const u32x4 w = qp[4 * s];
#pragma unroll
        for (int k = 0; k < 4; ++k) { qf[8 * s + 2 * k] = bf_lo(w[k]); qf[8 * s + 2 * k + 1] = bf_hi(w[k]); }
      }
    }
    float M = -INFINITY, lsum = 0.f;
    f32x4 O[8], Dc = {0.f, 0.f, 0.f, 0.f};
#pragma unroll
    for (int i = 0; i < 8; ++i) O[i] = f32x4{0.f, 0.f, 0.f, 0.f};

    const int Gbeg = NGK * pr + ((ci + NCONS - (NGK * pr) % NCONS) % NCONS);
    for (int G = Gbeg; G < NGK * (pr + 1); G += NCONS) {
      const int ps = (G & 1) * 4;
      const unsigned n0 = (unsigned)(4 * G);
      LAS unsigned char* meta = L + LDS_META + (G & 3) * 1024;
      wait_full(FULL, ps, n0 + 1, budget);
      u32x4 bq[4];
      f32x4 Dz = {0.f, 0.f, 0.f, 0.f};
#pragma unroll
      for (int s = 0; s < 4; ++s) {
        const u32x4 w = *(const LAS u32x4*)(meta + (32 * s + 8 * g) * 2);
        const u32x4 z = *(const LAS u32x4*)(meta + 256 + (32 * s + 8 * g) * 2);
#pragma unroll
        for (int k = 0; k < 4; ++k) bq[s][k] = pk_bf16(qf[8 * s + 2 * k] * bf_lo(w[k]), qf[8 * s + 2 * k + 1] * bf_hi(w[k]));
        Dz = mfma16(z, bq[s], Dz);
      }
      u32x2 vsw[4]; u32x4 vzf[2];
#pragma unroll
      for (int t = 0; t < 4; ++t) vsw[t] = *(const LAS u32x2*)(meta + 512 + (16 * t + 4 * g) * 2);
#pragma unroll
      for (int u = 0; u < 2; ++u) {
        const u32x2 lo = *(const LAS u32x2*)(meta + 640 + (32 * u + 4 * g) * 2);
        const u32x2 hi = *(const LAS u32x2*)(meta + 640 + (32 * u + 16 + 4 * g) * 2);
        vzf[u] = u32x4{lo[0], lo[1], hi[0], hi[1]};
      }
      f32x4 S[4];
#pragma unroll
      for (int u = 0; u < 2; ++u) {
        if (u == 1) wait_full(FULL, ps + 1, n0 + 2, budget);
        LAS unsigned char* slot = L + LDS_RING + (ps + u) * SLOT_BYTES;
#pragma unroll
        for (int tt = 0; tt < 2; ++tt) {
          i32x4 x[8];
#pragma unroll
          for (int i = 0; i < 8; ++i) x[i] = *(const LAS i32x4*)(slot + tt * 8192 + koff[i]);
          f32x4 acc = {0.f, 0.f, 0.f, 0.f};
#pragma unroll
          for (int s = 0; s < 4; ++s) {
            const i32x4 x0 = x[2 * s], x1 = x[2 * s + 1];
            const u32x4 af = {pk_bf16((float)x0[0], (float)x0[1]), pk_bf16((float)x0[2], (float)x0[3]),
                              pk_bf16((float)x1[0], (float)x1[1]), pk_bf16((float)x1[2], (float)x1[3])};
            acc = mfma16(af, bq[s], acc);
          }
          S[2 * u + tt] = acc;
        }
        wait_lgkm0();
        FREE[ps + u] = n0 + u + 1;
      }
      float gm = fmaxf(fmaxf(S[0][0], S[0][1]), fmaxf(S[0][2], S[0][3]));
#pragma unroll
      for (int t = 1; t < 4; ++t) gm = fmaxf(gm, fmaxf(fmaxf(S[t][0], S[t][1]), fmaxf(S[t][2], S[t][3])));
      const float dz = Dz[0];
      gm = xg_max(gm) - dz;
      const float Mn = fmaxf(M, gm);
      if (__any(Mn > M)) {
        const float alpha = __builtin_amdgcn_exp2f((M - Mn) * C2);
        lsum *= alpha;
#pragma unroll
        for (int i = 0; i < 8; ++i) O[i] *= alpha;
        Dc *= alpha;
        M = Mn;
      }
      const float bias = (M + dz) * C2;
      u32x4 pb[2];
#pragma unroll
      for (int t = 0; t < 4; ++t) {
        float p[4];
#pragma unroll
        for (int i = 0; i < 4; ++i) { p[i] = __builtin_amdgcn_exp2f(fmaf(S[t][i], C2, -bias)); lsum += p[i]; }
        pb[t >> 1][2 * (t & 1)]     = pk_bf16(p[0] * bf_lo(vsw[t][0]), p[1] * bf_hi(vsw[t][0]));
        pb[t >> 1][2 * (t & 1) + 1] = pk_bf16(p[2] * bf_lo(vsw[t][1]), p[3] * bf_hi(vsw[t][1]));
      }
#pragma unroll
      for (int u = 0; u < 2; ++u) {
        wait_full(FULL, ps + 2 + u, n0 + 3 + u, budget);
        LAS unsigned char* slot = L + LDS_RING + (ps + 2 + u) * SLOT_BYTES;
#pragma unroll
        for (int h = 0; h < 2; ++h) {
          i32x4 x[8];
#pragma unroll
          for (int j = 0; j < 8; ++j) x[j] = *(const LAS i32x4*)(slot + voff + ((j & 3) + 16 * (j >> 2)) * 512 + h * 256);
#pragma unroll
          for (int e = 0; e < 4; ++e) {
            const u32x4 af = {pk_bf16((float)x[0][e], (float)x[1][e]), pk_bf16((float)x[2][e], (float)x[3][e]),
                              pk_bf16((float)x[4][e], (float)x[5][e]), pk_bf16((float)x[6][e], (float)x[7][e])};
            O[4 * h + e] = mfma16(af, pb[u], O[4 * h + e]);
          }
        }
        Dc = mfma16(vzf[u], pb[u], Dc);
        wait_lgkm0();
        FREE[ps + 2 + u] = n0 + 3 + u;
      }
    }
    const float ltot = xg_sum(lsum);
    LAS unsigned char* rec = L + LDS_MRG + (pr * NCONS + ci) * MRG_REC;
    if (r < 8) {
      const float dc = Dc[0];
#pragma unroll
      for (int h = 0; h < 2; ++h)
#pragma unroll
        for (int i = 0; i < 4; ++i)
          *(LAS f32x4*)(rec + (r * DH + 64 * h + 16 * g + 4 * i) * 4) =
              f32x4{O[4 * h + 0][i] - dc, O[4 * h + 1][i] - dc, O[4 * h + 2][i] - dc, O[4 * h + 3][i] - dc};
      if (g == 0) { *(LAS float*)(rec + MROWS * DH * 4 + r * 4) = M; *(LAS float*)(rec + MROWS * DH * 4 + 32 + r * 4) = ltot; }
    }
    wait_lgkm0();
    unsigned old = 0;
    if (lane == 0) old = __hip_atomic_fetch_add(CNT + pr, 1u, __ATOMIC_RELAXED, __HIP_MEMORY_SCOPE_WORKGROUP);
    old = rfl(old);
    asm volatile("" ::: "memory");
    if (old == NCONS - 1) {
      const int m = lane >> 3, d0 = (lane & 7) * 16;
      LAS unsigned char* rb = L + LDS_MRG + pr * NCONS * MRG_REC;
      float Mc[NCONS], Lc[NCONS], Mx = -INFINITY;
#pragma unroll
      for (int c = 0; c < NCONS; ++c) {
        Mc[c] = *(const LAS float*)(rb + c * MRG_REC + MROWS * DH * 4 + m * 4);
        Lc[c] = *(const LAS float*)(rb + c * MRG_REC + MROWS * DH * 4 + 32 + m * 4);
        Mx = fmaxf(Mx, Mc[c]);
      }
      float w[NCONS], Lt = 0.f;
#pragma unroll
      for (int c = 0; c < NCONS; ++c) { w[c] = __builtin_amdgcn_exp2f((Mc[c] - Mx) * C2); Lt = fmaf(w[c], Lc[c], Lt); }
      const float inv = 1.f / Lt;
      u32x4 ov[2];
#pragma unroll
      for (int k = 0; k < 4; ++k) {
        f32x4 acc = {0.f, 0.f, 0.f, 0.f};
#pragma unroll
        for (int c = 0; c < NCONS; ++c) acc += *(const LAS f32x4*)(rb + c * MRG_REC + (m * DH + d0 + 4 * k) * 4) * w[c];
        acc *= inv;
        ov[k >> 1][2 * (k & 1)] = pk_bf16(acc[0], acc[1]);
        ov[k >> 1][2 * (k & 1) + 1] = pk_bf16(acc[2], acc[3]);
      }
      GAS u32x4* op = (GAS u32x4*)((char*)a.out + ((size_t)(pair * MROWS + m) * DH + d0) * 2);
      op[0] = ov[0]; op[1] = ov[1];
    }
  }
}

extern "C" void kernel_launch(void* const* d_in, const int* in_sizes, int n_in, void* d_out, int out_size, void* d_ws, size_t ws_size,
                              hipStream_t stream) {
  (void)d_ws; (void)ws_size;
  if (n_in != 7 || in_sizes[0] != NB * HQ * SQ * DH || in_sizes[1] != NPAIR * SKV * DH || in_sizes[2] != NPAIR * SKV * DH ||
      in_sizes[3] != NPAIR * NGK * DH || in_sizes[4] != NPAIR * NGK * DH || in_sizes[5] != NPAIR * SKV || in_sizes[6] != NPAIR * SKV ||
      out_size != NB * HQ * SQ * DH) {
    fprintf(stderr, "kernel_launch: unexpected shapes\n");
    return;
  }
  Args a;
  a.q = (const __hip_bfloat16*)d_in[0]; a.qk = (const int*)d_in[1]; a.qv = (const int*)d_in[2];
  a.ks = (const __hip_bfloat16*)d_in[3]; a.kz = (const __hip_bfloat16*)d_in[4];
  a.vs = (const __hip_bfloat16*)d_in[5]; a.vz = (const __hip_bfloat16*)d_in[6];
  a.out = (__hip_bfloat16*)d_out;
  kv_attn_kernel<<<NWG, NTHR, 0, stream>>>(a);
}
```

```cpp
#include <hip/hip_runtime.h>
#include <hip/hip_bf16.h>

#define LAS __attribute__((address_space(3)))
#define GAS __attribute__((address_space(1)))
typedef int      i32x4  __attribute__((ext_vector_type(4)));
typedef unsigned u32x4  __attribute__((ext_vector_type(4)));
typedef unsigned u32x2  __attribute__((ext_vector_type(2)));
typedef float    f32x4  __attribute__((ext_vector_type(4)));
typedef __bf16   bf16x8 __attribute__((ext_vector_type(8)));
typedef __bf16   bf16x2 __attribute__((ext_vector_type(2)));

#define KV_WIN 22
#define KV_WINU 20
#define KV_PACE 3562
#define KV_SLIP 20
#define KV_SLIPMAX 400
#define KV_WINW 32
#define KV_TWIDE 13500
#define KV_WSLIP 100
#define KV_NWIDE 160
#define KV_EARLY 3

constexpr int NB = 32, HQ = 32, SQ = 4, SKV = 2048, HKV = 16, DH = 128, GSZ = 64, NGK = SKV / GSZ;
constexpr int NPAIR = NB * HKV;
constexpr int MROWS = (HQ / HKV) * SQ;
constexpr int NWG = 256;
constexpr int PPW = NPAIR / NWG;
constexpr int NGRP = PPW * NGK;
constexpr int NCONS = 3;
constexpr int NTHR = (1 + NCONS) * 64;
constexpr int NSLOT = 8, SLOT_BYTES = 16384;
constexpr int LDS_RING = 0;
constexpr int LDS_META = NSLOT * SLOT_BYTES;
constexpr int MRG_REC = MROWS * DH * 4 + 64;
constexpr int LDS_MRG = LDS_META + 2 * NCONS * 1024;
constexpr int LDS_FLAG = LDS_MRG + PPW * 2 * MRG_REC;
constexpr int LDS_BYTES = LDS_FLAG + 128;
static_assert(LDS_BYTES <= 160 * 1024, "LDS");
constexpr int SPIN_CAP = 1 << 22;

template <int N> __device__ __forceinline__ void wait_vm() { asm volatile("s_waitcnt vmcnt(%0)" ::"i"(N) : "memory"); }
__device__ __forceinline__ void wait_lgkm0() { asm volatile("s_waitcnt lgkmcnt(0)" ::: "memory"); }
__device__ __forceinline__ void set_m0(unsigned v) { asm volatile("s_mov_b32 m0, %0\n\ts_nop 0" ::"s"(v) : "memory"); }
__device__ __forceinline__ void next_m0() { asm volatile("s_add_u32 m0, m0, 0x1000\n\ts_nop 0" ::: "memory", "scc"); }
template <int IMM> __device__ __forceinline__ void dma_buf(i32x4 rsrc, unsigned voff, unsigned soff) {
  asm volatile("buffer_load_dwordx4 %0, %1, %2 offen offset:%3 nt lds" ::"v"(voff), "s"(rsrc), "s"(soff), "i"(IMM) : "memory");
}
__device__ __forceinline__ void dma_glb(const void* p) {
  asm volatile("global_load_lds_dwordx4 %0, off nt" ::"v"(p) : "memory");
}
__device__ __forceinline__ unsigned rfl(unsigned v) { return __builtin_amdgcn_readfirstlane(v); }
__device__ __forceinline__ unsigned pk_bf16(float lo, float hi) {
  bf16x2 r = {(__bf16)lo, (__bf16)hi};
  return __builtin_bit_cast(unsigned, r);
}
__device__ __forceinline__ float bf_lo(unsigned w) { return __builtin_bit_cast(float, w << 16); }
__device__ __forceinline__ float bf_hi(unsigned w) { return __builtin_bit_cast(float, w & 0xffff0000u); }
__device__ __forceinline__ f32x4 mfma16(u32x4 a, u32x4 b, f32x4 c) {
  return __builtin_amdgcn_mfma_f32_16x16x32_bf16(__builtin_bit_cast(bf16x8, a), __builtin_bit_cast(bf16x8, b), c, 0, 0, 0);
}
__device__ __forceinline__ float max3f(float a, float b, float c) {
  float r; asm("v_max3_f32 %0, %1, %2, %3" : "=v"(r) : "v"(a), "v"(b), "v"(c)); return r;
}
__device__ __forceinline__ float xg_max(float x) {
  auto s = __builtin_amdgcn_permlane16_swap(__float_as_uint(x), __float_as_uint(x), false, false);
  x = fmaxf(__uint_as_float(s[0]), __uint_as_float(s[1]));
  auto t = __builtin_amdgcn_permlane32_swap(__float_as_uint(x), __float_as_uint(x), false, false);
  return fmaxf(__uint_as_float(t[0]), __uint_as_float(t[1]));
}
__device__ __forceinline__ float xg_sum(float x) {
  auto s = __builtin_amdgcn_permlane16_swap(__float_as_uint(x), __float_as_uint(x), false, false);
  x = __uint_as_float(s[0]) + __uint_as_float(s[1]);
  auto t = __builtin_amdgcn_permlane32_swap(__float_as_uint(x), __float_as_uint(x), false, false);
  return __uint_as_float(t[0]) + __uint_as_float(t[1]);
}

struct Args {
  const __hip_bfloat16* q; const int* qk; const int* qv;
  const __hip_bfloat16* ks; const __hip_bfloat16* kz; const __hip_bfloat16* vs; const __hip_bfloat16* vz;
  __hip_bfloat16* out;
};

__device__ __forceinline__ int swz(int row) { return (row & 1) | ((((row >> 1) & 1) << 2) ^ (((row >> 2) & 1) * 12)); }

constexpr int NSL = NGRP * 4;

template <int W, bool PACED>
__device__ __forceinline__ void load_slot(int n, i32x4 rs, const unsigned (&voff)[4], unsigned& soff, unsigned& dl, unsigned long long& tt, unsigned& slip, unsigned& fr,
                                          volatile LAS unsigned* FULL, volatile LAS unsigned* FREE) {
  static_assert(W >= 17 && W <= 32 && (KV_EARLY == 2 || KV_EARLY == 3), "window");
  constexpr int PPUB = (W - 1) & 15;
#pragma unroll
  for (int p = 0; p < 16; ++p) {
    if (PACED && (p & 3) == 0) {
      if (p != 0) asm volatile("s_waitcnt lgkmcnt(0)" : "+s"(tt)::"memory");
      dl += KV_PACE;
      const unsigned lo = ((unsigned)tt << 8) - KV_SLIP * 256u;
      if ((int)(lo - dl) > 0) { slip += lo - dl; dl = lo; }
      while ((int)(((unsigned)tt << 8) - dl) < 0) asm volatile("s_memrealtime %0\n\ts_waitcnt lgkmcnt(0)" : "=s"(tt)::"memory");
    }
    wait_vm<W - 1>();
    if ((p & 3) == 0) dma_buf<0>(rs, voff[0], soff);
    else if ((p & 3) == 1) dma_buf<1024>(rs, voff[1], soff);
    else if ((p & 3) == 2) dma_buf<2048>(rs, voff[2], soff);
    else {
      dma_buf<3072>(rs, voff[3], soff); soff += 4096;
      if (p != 15) next_m0();
    }
    if (PACED && (p & 3) == 1) asm volatile("s_memrealtime %0" : "=s"(tt)::"memory");
    if (PACED && p == 15) asm volatile("s_waitcnt lgkmcnt(0)" : "+s"(tt)::"memory");
    if (p == PPUB) {
      if (KV_EARLY == 3 && n == 3) FULL[0] = 1u;
      if (n >= 2) FULL[(n - 2) & 7] = (unsigned)(n - 1);
    }
    if (p == ((PPUB + 6) & 15)) fr = FREE[(n + 1) & 7];
  }
}

__device__ __forceinline__ void wait_full(volatile LAS unsigned* FULL, int ps, unsigned need, unsigned pre, int& budget) {
  if (rfl(pre) < need) {
    while (rfl(FULL[ps]) < need && budget > 0) { __builtin_amdgcn_s_sleep(1); --budget; }
  }
  asm volatile("" ::: "memory");
}

__global__ void __launch_bounds__(NTHR, 1) kv_attn_kernel(Args a) {
  __shared__ __attribute__((aligned(16))) unsigned char lds[LDS_BYTES];
  LAS unsigned char* L = (LAS unsigned char*)lds;
  volatile LAS unsigned* FULL = (volatile LAS unsigned*)(L + LDS_FLAG);
  volatile LAS unsigned* FREE = FULL + 8;
  LAS unsigned* CNT = (LAS unsigned*)(L + LDS_FLAG) + 16;
  const int tid = threadIdx.x, lane = tid & 63, wave = __builtin_amdgcn_readfirstlane(tid >> 6);
  const int wg = blockIdx.x;
  const unsigned ldsb = (unsigned)(size_t)L;

  if (__builtin_expect(wave == 0, 1)) {
    const unsigned long long kb = (unsigned long long)a.qk, kvx = kb ^ (unsigned long long)a.qv;
    i32x4 rs;
    rs.x = (int)(unsigned)kb; rs.y = (int)(unsigned)(kb >> 32); rs.z = NPAIR * SKV * DH * 4; rs.w = 0x00020000;
    unsigned voff[4];
#pragma unroll
    for (int i = 0; i < 4; ++i) voff[i] = (unsigned)((lane >> 5) * 512 + (((lane & 31) ^ swz(2 * i + (lane >> 5))) << 4));
    const int g0 = wg * NGRP;
    unsigned soff = (unsigned)g0 * (GSZ * DH * 4);
    set_m0(ldsb + LDS_RING);
#pragma unroll
    for (int p = 0; p < 16; p += 4) {
      dma_buf<0>(rs, voff[0], soff); dma_buf<1024>(rs, voff[1], soff); dma_buf<2048>(rs, voff[2], soff); dma_buf<3072>(rs, voff[3], soff);
      soff += 4096;
      if (p != 12) next_m0();
    }
    {
      i32x4 rv = rs; rv.x ^= (int)(unsigned)kvx; rv.y ^= (int)(unsigned)(kvx >> 32);
      set_m0(ldsb + LDS_META + 768);
      asm volatile("buffer_load_dword %0, %1, %2 offen nt lds" ::"v"((unsigned)lane * 4), "s"(rv), "s"((unsigned)g0 * (GSZ * DH * 4)) : "memory");
    }
    set_m0(ldsb + LDS_RING + SLOT_BYTES);
#pragma unroll 1
    for (int p = 16; p < 16 * KV_EARLY; p += 4) {
      dma_buf<0>(rs, voff[0], soff); dma_buf<1024>(rs, voff[1], soff); dma_buf<2048>(rs, voff[2], soff); dma_buf<3072>(rs, voff[3], soff);
      soff += 4096;
      next_m0();
    }
    int budget = SPIN_CAP;
    unsigned fr = 0;
    unsigned slip = 0;
    bool paced = KV_PACE != 0, wide = false;
    unsigned long long tt = __builtin_amdgcn_s_memrealtime();
    const unsigned tstart = (unsigned)tt;
    unsigned dl = (unsigned)tt << 8;
    __builtin_amdgcn_s_barrier();
#pragma unroll 1
    for (int n = KV_EARLY; n < NSL; ++n) {
      if (paced) load_slot<KV_WIN, true>(n, rs, voff, soff, dl, tt, slip, fr, FULL, FREE);
      else if (!wide) load_slot<KV_WINU, false>(n, rs, voff, soff, dl, tt, slip, fr, FULL, FREE);
      else load_slot<KV_WINW, false>(n, rs, voff, soff, dl, tt, slip, fr, FULL, FREE);
      if (KV_TWIDE != 0 && n >= KV_NWIDE && !wide) {
        if (!paced) tt = __builtin_amdgcn_s_memrealtime();
        if ((unsigned)tt - tstart > KV_TWIDE && (!paced || slip > KV_WSLIP * 256u)) { wide = true; paced = false; }
      }
      if (n == 7) slip = 0;
      if (slip > KV_SLIPMAX * 256u) paced = false;
      const int nx = n + 1;
      if ((nx & 3) == 0) {
        rs.x ^= (int)(unsigned)kvx; rs.y ^= (int)(unsigned)(kvx >> 32);
        if (nx & 4) soff -= 4 * SLOT_BYTES;
      }
      if (nx >= NSLOT) {
        const unsigned need = (unsigned)(nx - NSLOT + 1);
        if (rfl(fr) != need) {
          while (rfl(FREE[nx & 7]) != need && budget > 0) { __builtin_amdgcn_s_sleep(1); --budget; }
        }
      }
      set_m0(ldsb + LDS_RING + (nx & 7) * SLOT_BYTES);
    }
    wait_vm<16>();
    FULL[(NSL - 2) & 7] = (unsigned)(NSL - 1);
    wait_vm<0>();
    FULL[(NSL - 1) & 7] = (unsigned)NSL;
    return;
  }

  const int ci = wave - 1;
  const int r = lane & 15, g = lane >> 4;
  if (wave == 1 && lane < 32) ((LAS unsigned*)(L + LDS_FLAG))[lane] = 0u;
  wait_lgkm0();
  __builtin_amdgcn_s_barrier();
  asm volatile("" ::: "memory");

  constexpr float C2 = 0.08838834764831845f * 1.4426950408889634f;
  unsigned koff[8];
#pragma unroll
  for (int i = 0; i < 8; ++i) koff[i] = (unsigned)(r * 512 + (((8 * (i >> 1) + 2 * g + (i & 1)) ^ swz(r)) << 4));
  unsigned voff[4];
#pragma unroll
  for (int i = 0; i < 4; ++i) voff[i] = (unsigned)((4 * g + i) * 512 + ((r ^ swz(4 * g + i)) << 4));

#define PIN_O() asm volatile("" : "+a"(O[0]), "+a"(O[1]), "+a"(O[2]), "+a"(O[3]), "+a"(O[4]), "+a"(O[5]), "+a"(O[6]), "+a"(O[7]))
  const bool isk = lane < 32;
  const char* mb = isk ? (lane < 16 ? (const char*)a.ks : (const char*)a.kz) : ((lane >> 3) == 5 ? (const char*)a.vz : (const char*)a.vs);
  const unsigned mstride = isk ? 256u : 128u;
  const char* metap = mb + (size_t)(wg * NGRP + ci) * mstride + (isk ? (lane & 15) : (lane & 7)) * 16;
  unsigned mbuf = 0;
  set_m0(ldsb + LDS_META + ci * 2048);
  dma_glb(metap);
  const unsigned recoff = (unsigned)((g * 8 + (r & 7)) * 16);
  int budget = SPIN_CAP;
  for (int pr = 0; pr < PPW; ++pr) {
    const int pair = wg * PPW + pr;
    u32x4 qp4[4];
    {
      const GAS u32x4* qp = (const GAS u32x4*)((const char*)a.q + ((size_t)(pair * MROWS + (r & 7)) * DH + 8 * g) * 2);
#pragma unroll
      for (int s = 0; s < 4; ++s) qp4[s] = qp[4 * s];
    }
    float M = -INFINITY, lsum = 0.f;
    f32x4 O[8], Dc = {0.f, 0.f, 0.f, 0.f};
#pragma unroll
    for (int i = 0; i < 8; ++i) O[i] = f32x4{0.f, 0.f, 0.f, 0.f};

    const int Gbeg = NGK * pr + ((ci + NCONS - (NGK * pr) % NCONS) % NCONS);
    for (int G = Gbeg; G < NGK * (pr + 1); G += NCONS) {
      const int kp = (G & 1) * 2;
      const unsigned n0 = (unsigned)(8 * (G >> 1));
      PIN_O();
      const bool fin = (G == NGK * (pr + 1) - 1), mid = (G == NGK * (pr + 1) - 2);
      LAS unsigned char* rec0 = L + LDS_MRG + (pr * 2) * MRG_REC;
      volatile LAS unsigned* cnt = CNT + pr;
      LAS unsigned char* meta = L + LDS_META + ci * 2048 + mbuf;
      wait_vm<0>();
      mbuf ^= 1024;
      if (G + NCONS < NGRP) {
        metap += NCONS * mstride;
        set_m0(ldsb + LDS_META + ci * 2048 + mbuf);
        dma_glb(metap);
      }
      u32x4 bq[4];
      f32x4 Dz = {0.f, 0.f, 0.f, 0.f};
#pragma unroll
      for (int s = 0; s < 4; ++s) {
        const u32x4 w = *(const LAS u32x4*)(meta + (32 * s + 8 * g) * 2);
        const u32x4 z = *(const LAS u32x4*)(meta + 256 + (32 * s + 8 * g) * 2);
#pragma unroll
        for (int k = 0; k < 4; ++k) bq[s][k] = pk_bf16(bf_lo(qp4[s][k]) * bf_lo(w[k]), bf_hi(qp4[s][k]) * bf_hi(w[k]));
        Dz = mfma16(z, bq[s], Dz);
      }
      float dz = Dz[0];
      u32x2 vsw[4]; u32x4 vzf[2];
#pragma unroll
      for (int t = 0; t < 4; ++t) vsw[t] = *(const LAS u32x2*)(meta + 512 + (16 * t + 4 * g) * 2);
#pragma unroll
      for (int u = 0; u < 2; ++u) {
        const u32x2 lo = *(const LAS u32x2*)(meta + 640 + (32 * u + 4 * g) * 2);
        const u32x2 hi = *(const LAS u32x2*)(meta + 640 + (32 * u + 16 + 4 * g) * 2);
        vzf[u] = u32x4{lo[0], lo[1], hi[0], hi[1]};
      }
      asm volatile("" : "+v"(bq[0]), "+v"(bq[1]), "+v"(bq[2]), "+v"(bq[3]), "+v"(dz), "+v"(vsw[0]), "+v"(vsw[1]), "+v"(vsw[2]), "+v"(vsw[3]), "+v"(vzf[0]), "+v"(vzf[1]));
      unsigned pre = 0;
      f32x4 S[4];
#pragma unroll
      for (int u = 0; u < 2; ++u) {
        wait_full(FULL, kp + u, n0 + kp + u + 1, pre, budget);
        LAS unsigned char* slot = L + LDS_RING + (kp + u) * SLOT_BYTES;
        i32x4 x[2][8];
#pragma unroll
        for (int tt = 0; tt < 2; ++tt)
#pragma unroll
          for (int i = 0; i < 8; ++i) x[tt][i] = *(const LAS i32x4*)(slot + tt * 8192 + koff[i]);
        if (u == 0) pre = FULL[kp + 1];
#pragma unroll
        for (int tt = 0; tt < 2; ++tt) {
          f32x4 acc = {0.f, 0.f, 0.f, 0.f};
#pragma unroll
          for (int s = 0; s < 4; ++s) {
            const i32x4 x0 = x[tt][2 * s], x1 = x[tt][2 * s + 1];
            const u32x4 af = {pk_bf16((float)x0[0], (float)x0[1]), pk_bf16((float)x0[2], (float)x0[3]),
                              pk_bf16((float)x1[0], (float)x1[1]), pk_bf16((float)x1[2], (float)x1[3])};
            acc = mfma16(af, bq[s], acc);
          }
          S[2 * u + tt] = acc;
        }
        wait_lgkm0();
        FREE[kp + u] = n0 + kp + u + 1;
      }
      if (mid) {
        while (rfl(*cnt) < 1 && budget > 0) { __builtin_amdgcn_s_sleep(1); --budget; }
        asm volatile("" ::: "memory");
        const float Mc = *(const LAS float*)(rec0 + MROWS * DH * 4 + (r & 7) * 4);
        const float Lc = *(const LAS float*)(rec0 + MROWS * DH * 4 + 32 + (r & 7) * 4);
        const float Mx = fmaxf(M, Mc);
        const float alpha = __builtin_amdgcn_exp2f((M - Mx) * C2), w = __builtin_amdgcn_exp2f((Mc - Mx) * C2);
        lsum = fmaf(w * 0.25f, Lc, lsum * alpha);
        Dc *= alpha;
        M = Mx;
#pragma unroll
        for (int h = 0; h < 2; ++h)
#pragma unroll
          for (int i = 0; i < 4; ++i) {
            const f32x4 t = *(const LAS f32x4*)(rec0 + (h * 4 + i) * 512 + recoff);
#pragma unroll
            for (int e = 0; e < 4; ++e) O[4 * h + e][i] = fmaf(t[e], w, O[4 * h + e][i] * alpha);
          }
        PIN_O();
      }
      float m4 = max3f(S[0][0], S[0][1], S[0][2]);
      m4 = max3f(m4, S[0][3], S[1][0]); m4 = max3f(m4, S[1][1], S[1][2]); m4 = max3f(m4, S[1][3], S[2][0]);
      m4 = max3f(m4, S[2][1], S[2][2]); m4 = max3f(m4, S[2][3], S[3][0]); m4 = max3f(m4, S[3][1], S[3][2]);
      m4 = fmaxf(m4, S[3][3]);
      const float Mn = fmaxf(M, xg_max(m4 - dz));
      if (__any(Mn > M)) {
        const float alpha = __builtin_amdgcn_exp2f((M - Mn) * C2);
        lsum *= alpha;
        Dc *= alpha;
#pragma unroll
        for (int i = 0; i < 8; ++i) O[i] *= alpha;
        M = Mn;
      }
      PIN_O();
      const float bias = (M + dz) * C2;
      u32x4 pb[2];
#pragma unroll
      for (int t = 0; t < 4; ++t) {
        float p[4];
#pragma unroll
        for (int i = 0; i < 4; ++i) { p[i] = __builtin_amdgcn_exp2f(fmaf(S[t][i], C2, -bias)); lsum += p[i]; }
        pb[t >> 1][2 * (t & 1)]     = pk_bf16(p[0] * bf_lo(vsw[t][0]), p[1] * bf_hi(vsw[t][0]));
        pb[t >> 1][2 * (t & 1) + 1] = pk_bf16(p[2] * bf_lo(vsw[t][1]), p[3] * bf_hi(vsw[t][1]));
      }
      Dc = mfma16(vzf[0], pb[0], Dc);
      Dc = mfma16(vzf[1], pb[1], Dc);
      asm volatile("" : "+v"(pb[0]), "+v"(pb[1]), "+v"(lsum));
      pre = 0;
      float w0 = 0.f;
      f32x4 X[8];
#pragma unroll
      for (int u = 0; u < 2; ++u) {
        if (u == 1 && fin) {
          while (rfl(*cnt) < 2 && budget > 0) { __builtin_amdgcn_s_sleep(1); --budget; }
          asm volatile("" ::: "memory");
          LAS unsigned char* rec1 = rec0 + MRG_REC;
          const float Mc = *(const LAS float*)(rec1 + MROWS * DH * 4 + (r & 7) * 4);
          const float Lc = *(const LAS float*)(rec1 + MROWS * DH * 4 + 32 + (r & 7) * 4);
#pragma unroll
          for (int hi = 0; hi < 8; ++hi) X[hi] = *(const LAS f32x4*)(rec1 + hi * 512 + recoff);
          const float Mx = fmaxf(M, Mc);
          w0 = __builtin_amdgcn_exp2f((M - Mx) * C2);
          float w1 = __builtin_amdgcn_exp2f((Mc - Mx) * C2);
          const float inv = 1.f / fmaf(w0, xg_sum(lsum), w1 * Lc);
          w0 *= inv; w1 *= inv;
          const float c0 = -Dc[0] * w0;
#pragma unroll
          for (int hi = 0; hi < 8; ++hi)
#pragma unroll
            for (int e = 0; e < 4; ++e) X[hi][e] = fmaf(X[hi][e], w1, c0);
          asm volatile("" : "+v"(X[0]), "+v"(X[1]), "+v"(X[2]), "+v"(X[3]), "+v"(X[4]), "+v"(X[5]), "+v"(X[6]), "+v"(X[7]), "+v"(w0));
        }
        wait_full(FULL, 4 + kp + u, n0 + 5 + kp + u, pre, budget);
        LAS unsigned char* slot = L + LDS_RING + (4 + kp + u) * SLOT_BYTES;
        i32x4 x[2][8];
#pragma unroll
        for (int h = 0; h < 2; ++h)
#pragma unroll
          for (int j = 0; j < 8; ++j) x[h][j] = *(const LAS i32x4*)(slot + voff[j & 3] + (j >> 2) * 8192 + h * 256);
        if (u == 0) pre = FULL[5 + kp];
#pragma unroll
        for (int h = 0; h < 2; ++h) {
#pragma unroll
          for (int e = 0; e < 4; ++e) {
            const u32x4 af = {pk_bf16((float)x[h][0][e], (float)x[h][1][e]), pk_bf16((float)x[h][2][e], (float)x[h][3][e]),
                              pk_bf16((float)x[h][4][e], (float)x[h][5][e]), pk_bf16((float)x[h][6][e], (float)x[h][7][e])};
            O[4 * h + e] = mfma16(af, pb[u], O[4 * h + e]);
          }
        }
        wait_lgkm0();
        FREE[4 + kp + u] = n0 + 5 + kp + u;
        PIN_O();
      }
      if (fin) {
        GAS u32x4* op = (GAS u32x4*)((char*)a.out + ((size_t)(pair * MROWS + (r & 7)) * DH + 16 * g) * 2);
#pragma unroll
        for (int h = 0; h < 2; ++h) {
          u32x4 ov[2];
#pragma unroll
          for (int i = 0; i < 4; ++i) {
            const f32x4 x4 = X[h * 4 + i];
            ov[i >> 1][2 * (i & 1)] = pk_bf16(fmaf(O[4 * h + 0][i], w0, x4[0]), fmaf(O[4 * h + 1][i], w0, x4[1]));
            ov[i >> 1][2 * (i & 1) + 1] = pk_bf16(fmaf(O[4 * h + 2][i], w0, x4[2]), fmaf(O[4 * h + 3][i], w0, x4[3]));
          }
          if (r < 8) { op[8 * h] = ov[0]; op[8 * h + 1] = ov[1]; }
        }
      }
    }
    if ((NGK * (pr + 1) - 1) % NCONS != ci) {
      const float ltot = xg_sum(lsum), dc = Dc[0];
      LAS unsigned char* rec = L + LDS_MRG + (pr * 2 + ((NGK * (pr + 1) - 2) % NCONS == ci ? 1 : 0)) * MRG_REC;
      if (r < 8) {
#pragma unroll
        for (int h = 0; h < 2; ++h)
#pragma unroll
          for (int i = 0; i < 4; ++i)
            *(LAS f32x4*)(rec + (h * 4 + i) * 512 + recoff) = f32x4{O[4 * h + 0][i] - dc, O[4 * h + 1][i] - dc, O[4 * h + 2][i] - dc, O[4 * h + 3][i] - dc};
        if (g == 0) { *(LAS float*)(rec + MROWS * DH * 4 + r * 4) = M; *(LAS float*)(rec + MROWS * DH * 4 + 32 + r * 4) = ltot; }
      }
      wait_lgkm0();
      if (lane == 0) __hip_atomic_fetch_add(CNT + pr, 1u, __ATOMIC_RELAXED, __HIP_MEMORY_SCOPE_WORKGROUP);
    }
  }
}

extern "C" void kernel_launch(void* const* d_in, const int* in_sizes, int n_in, void* d_out, int out_size, void* d_ws, size_t ws_size,
                              hipStream_t stream) {
  (void)d_ws; (void)ws_size;
  if (n_in != 7 || in_sizes[0] != NB * HQ * SQ * DH || in_sizes[1] != NPAIR * SKV * DH || in_sizes[2] != NPAIR * SKV * DH ||
      in_sizes[3] != NPAIR * NGK * DH || in_sizes[4] != NPAIR * NGK * DH || in_sizes[5] != NPAIR * SKV || in_sizes[6] != NPAIR * SKV ||
      out_size != NB * HQ * SQ * DH)
    return;
  Args a;
  a.q = (const __hip_bfloat16*)d_in[0]; a.qk = (const int*)d_in[1]; a.qv = (const int*)d_in[2];
  a.ks = (const __hip_bfloat16*)d_in[3]; a.kz = (const __hip_bfloat16*)d_in[4];
  a.vs = (const __hip_bfloat16*)d_in[5]; a.vz = (const __hip_bfloat16*)d_in[6];
  a.out = (__hip_bfloat16*)d_out;
  kv_attn_kernel<<<NWG, NTHR, 0, stream>>>(a);
}
```
